```python
import torch

def _softmax(x, axis=-1):
    return torch.softmax(x, dim=axis)
def _gelu(x):
    return torch.nn.functional.gelu(x, approximate="tanh")
def _silu(x):
    return torch.nn.functional.silu(x)
def _sigmoid(x):
    return torch.sigmoid(x)
def _layernorm(x, axis=-1, eps=1e-5):
    m = x.mean(dim=axis, keepdim=True)
    v = x.var(dim=axis, keepdim=True, unbiased=False)
    return (x - m) / torch.sqrt(v + eps)
def _scatter_add(tgt, idx, src, axis=0):
    return tgt.clone().index_add_(axis, idx, src)
def _segment_sum(src, seg, n):
    out = torch.zeros((n,) + src.shape[1:], dtype=src.dtype)
    return out.index_add_(0, seg, src)
def _pad(x, pw):
    flat = tuple(v for pair in reversed(pw) for v in pair)
    return torch.nn.functional.pad(x, flat)

kernel_name = 'sequential_preLN__compressed_kv_cla__glu_gated_reglu__decode'

def setup_inputs():

    _w = (lambda a: (lambda t: t.to(torch.bfloat16) if t.is_floating_point() else t)(torch.as_tensor(a)))
    def _A_setup():
        B, H, Sq, Sk, D, M = 4, 32, 1, 4096, 128, 512
        Dc = 4 * D
        return {
            "x": _w((lambda *s: torch.randn(*s, dtype=torch.bfloat16) * 0.05)(B, Sq, M)),
            "c_kv": _w((lambda *s: torch.randn(*s, dtype=torch.bfloat16) * 0.8)(B, Sk, Dc)),
            "w_q": _w((lambda *s: torch.randn(*s, dtype=torch.bfloat16) * 1.6)(M, H, D) * (M ** -0.5)),
            "w_uk": _w((lambda *s: torch.randn(*s, dtype=torch.bfloat16) * 1.6)(Dc, H, D) * (Dc ** -0.5)),
            "w_uv": _w((lambda *s: torch.randn(*s, dtype=torch.bfloat16) * 0.8)(Dc, H, D) * (Dc ** -0.5)),
            "w_o": _w((lambda *s: torch.randn(*s, dtype=torch.bfloat16) * 0.8)(H, D, M) * ((H * D) ** -0.5)),
        }
    _A_in = _A_setup()

    def _B_setup():
        B, S, D, F = 4, 1, 512, 1792
        return {
            "x": _w((lambda *s: torch.randn(*s, dtype=torch.bfloat16) * 0.05)(B, S, D)),
            "W_gate": _w((lambda *s: torch.randn(*s, dtype=torch.bfloat16) * 0.4)(D, F) * (2.0 / D) ** 0.5),
            "W_up": _w((lambda *s: torch.randn(*s, dtype=torch.bfloat16) * 0.4)(D, F) * (2.0 / D) ** 0.5),
            "W_down": _w((lambda *s: torch.randn(*s, dtype=torch.bfloat16) * 0.4)(F, D) * (1.0 / F) ** 0.5),
        }
    _B_in = _B_setup()

    return {
        "A_x": _A_in["x"],
        "A_c_kv": _A_in["c_kv"],
        "A_w_q": _A_in["w_q"],
        "A_w_uk": _A_in["w_uk"],
        "A_w_uv": _A_in["w_uv"],
        "A_w_o": _A_in["w_o"],
        "B_x": _B_in["x"],
        "B_W_gate": _B_in["W_gate"],
        "B_W_up": _B_in["W_up"],
        "B_W_down": _B_in["W_down"],
    }

def reference(A_x, A_c_kv, A_w_q, A_w_uk, A_w_uv, A_w_o, B_x, B_W_gate, B_W_up, B_W_down):
    def _A_ref(x, c_kv, w_q, w_uk, w_uv, w_o):
        B, Sq, M = x.shape[0], x.shape[1], x.shape[2]
        Sk = c_kv.shape[1]
        H, D = w_q.shape[1], w_q.shape[2]
        q = torch.einsum("bqm,mhd->bhqd", x, w_q)
        k = torch.einsum("bkc,chd->bhkd", c_kv, w_uk)
        v = torch.einsum("bkc,chd->bhkd", c_kv, w_uv)
        scores = torch.einsum("bhqd,bhkd->bhqk", q, k) * (D ** -0.5)
        off = Sk - Sq
        qi = torch.arange(Sq)[:, None]
        kj = torch.arange(Sk)[None, :]
        cmask = kj <= (qi + off)
        scores = torch.where(cmask[None, None], scores, -1e9)
        p = _softmax(scores, axis=-1)
        ctx = torch.einsum("bhqk,bhkd->bhqd", p, v)
        return torch.einsum("bhqd,hdm->bqm", ctx, w_o)

    def _B_ref(x, W_gate, W_up, W_down):
        gate = torch.einsum("bsd,df->bsf", x, W_gate)
        up = torch.einsum("bsd,df->bsf", x, W_up)
        gate = gate * (lambda x, dt: x.to(dt))(gate > 0, gate.dtype)
        h = gate * up
        return torch.einsum("bsf,fd->bsd", h, W_down)

    x = A_x
    h = x + _A_ref(_layernorm(x, axis=-1), A_c_kv, A_w_q, A_w_uk, A_w_uv, A_w_o)
    h2 = h + _B_ref(_layernorm(h, axis=-1), B_W_gate, B_W_up, B_W_down)
    return h2
```

```cpp
#include <hip/hip_runtime.h>
#include <hip/hip_bf16.h>
typedef unsigned short u16;
__device__ __forceinline__ float bf2f(u16 v){ return __uint_as_float(((unsigned)v)<<16); }
__device__ __forceinline__ u16 f2bf(float f){ __hip_bfloat16 b = __float2bfloat16(f); return *(u16*)&b; }
constexpr int B=4,H=32,SK=4096,D=128,M=512,DC=512,F=1792;
constexpr int NS=64;
__global__ __launch_bounds__(256) void k1(const u16* x,const u16* wq,const u16* wuk,float* qabs){
  __shared__ float xn[B][M]; __shared__ float q[B][D]; __shared__ float red[2][B][D];
  int h=blockIdx.x>>3, j=blockIdx.x&7, t=threadIdx.x, w=t>>6, l=t&63;
  { int b=w; float v[8]; float s=0; for(int i=0;i<8;i++){ v[i]=bf2f(x[b*M+l*8+i]); s+=v[i]; }
    for(int o=32;o;o>>=1) s+=__shfl_xor(s,o); float m=s/M; float ss=0; for(int i=0;i<8;i++){ float d=v[i]-m; ss+=d*d; }
    for(int o=32;o;o>>=1) ss+=__shfl_xor(ss,o); float r=1.0f/sqrtf(ss/M+1e-5f);
    for(int i=0;i<8;i++) xn[b][l*8+i]=(v[i]-m)*r; }
  __syncthreads();
  { int d=t&127, half=t>>7; float a[B]={0,0,0,0};
    for(int m=half*256;m<half*256+256;m++){ float wv=bf2f(wq[(size_t)m*H*D+h*D+d]); for(int b=0;b<B;b++) a[b]+=xn[b][m]*wv; }
    for(int b=0;b<B;b++) red[half][b][d]=a[b]; }
  __syncthreads();
  for(int i=t;i<B*D;i+=256){ int b=i>>7,d=i&127; q[b][d]=red[0][b][d]+red[1][b][d]; }
  __syncthreads();
  { int c=j*64+(t>>2), part=t&3; float a[B]={0,0,0,0};
    for(int d=part*32;d<part*32+32;d++){ float wv=bf2f(wuk[(size_t)c*H*D+h*D+d]); for(int b=0;b<B;b++) a[b]+=q[b][d]*wv; }
    for(int b=0;b<B;b++){ a[b]+=__shfl_xor(a[b],1); a[b]+=__shfl_xor(a[b],2); }
    if(part==0) for(int b=0;b<B;b++) qabs[(b*H+h)*DC+c]=a[b]*0.08838834764831845f; }
}
__global__ __launch_bounds__(256) void k2(const u16* ckv,const float* qabs,float* op,float* lse){
  __shared__ u16 c[64][DC+8]; __shared__ float s[H][64+1];
  int b=blockIdx.x>>6, sp=blockIdx.x&63, t=threadIdx.x;
  const u16* src=ckv+((size_t)b*SK+sp*64)*DC;
  for(int i=t;i<64*DC/8;i+=256){ int r=i>>6, cc=(i&63)*8; *(uint4*)&c[r][cc]=*(const uint4*)&src[r*DC+cc]; }
  __syncthreads();
  { int k=t&63, hg=t>>6; for(int hh=0;hh<8;hh++){ int h=hg*8+hh; const float* qp=qabs+(b*H+h)*DC; float a=0;
      for(int i=0;i<DC;i++) a+=qp[i]*bf2f(c[k][i]); s[h][k]=a; } }
  __syncthreads();
  { int l=t&63, w=t>>6; for(int hh=0;hh<8;hh++){ int h=w*8+hh; float v=s[h][l]; float m=v; for(int o=32;o;o>>=1) m=fmaxf(m,__shfl_xor(m,o));
      float p=__expf(v-m); float su=p; for(int o=32;o;o>>=1) su+=__shfl_xor(su,o); s[h][l]=p/su; if(l==0) lse[(b*H+h)*NS+sp]=m+__logf(su); } }
  __syncthreads();
  { for(int h=0;h<H;h++){ float a0=0,a1=0; for(int k=0;k<64;k++){ float p=s[h][k]; unsigned v=*(const unsigned*)&c[k][t*2]; a0+=p*__uint_as_float(v<<16); a1+=p*__uint_as_float(v&0xffff0000u);}
      float2 o={a0,a1}; *(float2*)&op[(((size_t)(b*H+h))*NS+sp)*DC+t*2]=o; } }
}
__global__ __launch_bounds__(256) void k3(const float* op,const float* lse,const u16* wuv,float* ctx){
  __shared__ float wgt[NS]; __shared__ float cc[DC]; __shared__ float red[2][D];
  int bh=blockIdx.x, h=bh&31, t=threadIdx.x;
  if(t<64){ float v=lse[bh*NS+t]; float m=v; for(int o=32;o;o>>=1) m=fmaxf(m,__shfl_xor(m,o)); float p=__expf(v-m); float su=p; for(int o=32;o;o>>=1) su+=__shfl_xor(su,o); wgt[t]=p/su; }
  __syncthreads();
  { float a0=0,a1=0; for(int s=0;s<NS;s++){ float2 v=*(const float2*)&op[((size_t)bh*NS+s)*DC+t*2]; a0+=wgt[s]*v.x; a1+=wgt[s]*v.y; } cc[t*2]=a0; cc[t*2+1]=a1; }
  __syncthreads();
  { int d=t&127, half=t>>7; float a=0; for(int c=half*256;c<half*256+256;c++) a+=cc[c]*bf2f(wuv[(size_t)c*H*D+h*D+d]); red[half][d]=a; }
  __syncthreads();
  if(t<128) ctx[bh*D+t]=red[0][t]+red[1][t];
}
__global__ __launch_bounds__(256) void k4(const float* ctx,const u16* wo,float* part){
  int k0=blockIdx.x*64, t=threadIdx.x; float a[B][2]={};
  for(int k=k0;k<k0+64;k++){ unsigned v=*(const unsigned*)&wo[(size_t)k*M+t*2]; float w0=__uint_as_float(v<<16), w1=__uint_as_float(v&0xffff0000u);
    for(int b=0;b<B;b++){ float cv=ctx[b*H*D+k]; a[b][0]+=cv*w0; a[b][1]+=cv*w1; } }
  for(int b=0;b<B;b++){ part[(blockIdx.x*B+b)*M+t*2]=a[b][0]; part[(blockIdx.x*B+b)*M+t*2+1]=a[b][1]; }
}
__global__ __launch_bounds__(256) void k5(const u16* x,const float* part,const u16* wg,const u16* wu,const u16* wd,float* hbuf,float* part2){
  __shared__ float hn[B][M]; __shared__ float red[8][2][B][32]; __shared__ float mid[B][32];
  int t=threadIdx.x,w=t>>6,l=t&63;
  { int b=w; float v[8]; float s=0; for(int i=0;i<8;i++){ float a=0; for(int p=0;p<64;p++) a+=part[(p*B+b)*M+l*8+i]; v[i]=bf2f(x[b*M+l*8+i])+a; s+=v[i]; }
    if(blockIdx.x==0) for(int i=0;i<8;i++) hbuf[b*M+l*8+i]=v[i];
    for(int o=32;o;o>>=1) s+=__shfl_xor(s,o); float m=s/M; float ss=0; for(int i=0;i<8;i++){ float d=v[i]-m; ss+=d*d; }
    for(int o=32;o;o>>=1) ss+=__shfl_xor(ss,o); float r=1.0f/sqrtf(ss/M+1e-5f);
    for(int i=0;i<8;i++) hn[b][l*8+i]=(v[i]-m)*r; }
  __syncthreads();
  int f0=blockIdx.x*32;
  { int f=t&31, kp=t>>5; float g[B]={},u[B]={};
    for(int m=kp*64;m<kp*64+64;m++){ float a=bf2f(wg[(size_t)m*F+f0+f]), c=bf2f(wu[(size_t)m*F+f0+f]); for(int b=0;b<B;b++){ g[b]+=hn[b][m]*a; u[b]+=hn[b][m]*c; } }
    for(int b=0;b<B;b++){ red[kp][0][b][f]=g[b]; red[kp][1][b][f]=u[b]; } }
  __syncthreads();
  if(t<128){ int b=t>>5,f=t&31; float g=0,u=0; for(int k=0;k<8;k++){ g+=red[k][0][b][f]; u+=red[k][1][b][f]; } mid[b][f]=(g>0?g:0.f)*u; }
  __syncthreads();
  { float a[B][2]={}; for(int f=0;f<32;f++){ unsigned v=*(const unsigned*)&wd[(size_t)(f0+f)*M+t*2]; float w0=__uint_as_float(v<<16), w1=__uint_as_float(v&0xffff0000u);
      for(int b=0;b<B;b++){ a[b][0]+=mid[b][f]*w0; a[b][1]+=mid[b][f]*w1; } }
    for(int b=0;b<B;b++){ part2[(blockIdx.x*B+b)*M+t*2]=a[b][0]; part2[(blockIdx.x*B+b)*M+t*2+1]=a[b][1]; } }
}
__global__ __launch_bounds__(256) void k6(const float* hbuf,const float* part2,u16* out){
  int i=blockIdx.x*256+threadIdx.x; float a=hbuf[i]; float s=0; for(int p=0;p<56;p++) s+=part2[p*B*M+i]; out[i]=f2bf(a+s);
}
extern "C" void kernel_launch(void* const* d_in,const int* in_sizes,int n_in,void* d_out,int out_size,void* d_ws,size_t ws_size,hipStream_t stream){
  const u16* x=(const u16*)d_in[0]; const u16* ckv=(const u16*)d_in[1]; const u16* wq=(const u16*)d_in[2]; const u16* wuk=(const u16*)d_in[3];
  const u16* wuv=(const u16*)d_in[4]; const u16* wo=(const u16*)d_in[5]; const u16* wg=(const u16*)d_in[7]; const u16* wu=(const u16*)d_in[8]; const u16* wd=(const u16*)d_in[9];
  float* ws=(float*)d_ws; float* qabs=ws; float* lse=qabs+B*H*DC; float* ctx=lse+B*H*NS; float* part=ctx+B*H*D; float* hbuf=part+64*B*M; float* part2=hbuf+B*M; float* op=part2+64*B*M;
  k1<<<256,256,0,stream>>>(x,wq,wuk,qabs);
  k2<<<256,256,0,stream>>>(ckv,qabs,op,lse);
  k3<<<128,256,0,stream>>>(op,lse,wuv,ctx);
  k4<<<64,256,0,stream>>>(ctx,wo,part);
  k5<<<56,256,0,stream>>>(x,part,wg,wu,wd,hbuf,part2);
  k6<<<8,256,0,stream>>>(hbuf,part2,(u16*)d_out);
}
```

```cpp
#define CODE_LINES 156
#define ZGU 0
#define DL_PF 20
#include <hip/hip_runtime.h>
#include <hip/hip_bf16.h>

#define NTW 0
#define NTC 0
#define DL_H1 0
#define DL_ML 0
#define DL_H2 0
#define DL_HA 12
#define DL_HB 20
#define DL_HV 10
#define DL_T4 4
#define DL_DW 0
#define DL_CK 0
#define KTH 8
#define GO_AT 31
#define DL_WO 24
#define DL_FL 2
#define DL_WV 0
#define DL_MR 2
#define RETOUCH 0
#define DL_WD 0
#define PFIRST 1
#define TLBW 3
#define PFA1 1
#define DL_HR 4
#define T_H2 170
#define D_HA 45
#define D_HB 130
#define D_T4 135
#define WO_S 0
#define DL_RT 3
#define HI_AT 1
#define T_H1 130
#define H_A 90
#define H_B 60
#define SPLIT_S 16
#define PFWO 0
#define ZWO 1
#define ZWD 0
#define T_Q 70
#define WO_N 4
#define KPRE 4
#define WV_A 3
#define WV_S 3
#define ABL 0
#define PF0 1
#define TSTR (1 << 19)
#define KAT 12

namespace {
#define GAS __attribute__((address_space(1)))
#define LAS __attribute__((address_space(3)))
#define DI __device__ __forceinline__
#define FENCE() asm volatile("" ::: "memory")
#define NOUNROLL _Pragma("clang loop unroll(disable)")
typedef unsigned short u16;
typedef unsigned long long u64;
typedef unsigned v4u __attribute__((ext_vector_type(4)));
typedef unsigned v2u __attribute__((ext_vector_type(2)));
typedef float f32x4 __attribute__((ext_vector_type(4)));
typedef float f32x2 __attribute__((ext_vector_type(2)));
typedef short s16x4 __attribute__((ext_vector_type(4)));
typedef __bf16 bf16x2 __attribute__((ext_vector_type(2)));
typedef __bf16 bf16x8 __attribute__((ext_vector_type(8)));
typedef __amdgpu_buffer_rsrc_t rsrc_t;
typedef u64 u64x8 __attribute__((ext_vector_type(8)));
typedef u64 u64x2 __attribute__((ext_vector_type(2)));

constexpr unsigned SENT = 0xFFFFFFFFu;
constexpr int SC1 = 16;
constexpr int SPIN_CAP = 40000;
constexpr int WS_E = 0, WS_FLAG = 4096, WS_FLG2 = WS_FLAG + 4096, WS_QA = WS_FLG2 + 4096, WS_QK = WS_QA + 262144, WS_ML = WS_QK + 16384, WS_CK = WS_ML + 65536, WS_HA = WS_CK + 262144,
              WS_HB = WS_HA + 131072, WS_FO = WS_HB + 262144, WS_OP = WS_FO + 112 * 8192, OPS = 16384 + 128, WS_END = WS_OP + 256 * OPS;
constexpr int WS_STAMP = 8 << 20;
constexpr int OOB = 0x7FFFFF00;
constexpr int L_C = 0, L_Q = 131072, L_P = L_Q + 16384, L_QP = L_P + 4096, L_MX = L_QP + 4096, L_CNT = L_MX + 256, L_WG = L_CNT + 128, L_SINK = L_WG + 512, L_EV = L_SINK + 256, L_XN = L_EV + 256, L_END = L_XN + 4096;
constexpr int L_CX = 8192, L_R2 = 12288, L_VS = 14336, L_HO = 14592, L_GU = 0, L_CS = 4096, L_WO = 16384, L_FG = 32768, L_FD = 65536;

DI rsrc_t mk(u64 p, int bytes) { return __builtin_amdgcn_make_buffer_rsrc((void*)p, 0, bytes, 0x00020000); }
template <int AUX> DI v4u ld4(rsrc_t r, int v, int s = 0) { return __builtin_amdgcn_raw_buffer_load_b128(r, v, s, AUX); }
template <int AUX> DI v2u ld2(rsrc_t r, int v, int s = 0) { return __builtin_amdgcn_raw_buffer_load_b64(r, v, s, AUX); }
template <int AUX> DI unsigned ld1(rsrc_t r, int v, int s = 0) { return __builtin_amdgcn_raw_buffer_load_b32(r, v, s, AUX); }
DI void st4(rsrc_t r, v4u x, int v, int imm = 0) { __builtin_amdgcn_raw_buffer_store_b128(x, r, v + imm, 0, SC1); }
DI void st2(rsrc_t r, v2u x, int v) { __builtin_amdgcn_raw_buffer_store_b64(x, r, v, 0, SC1); }
DI float asf(unsigned u) { return __builtin_bit_cast(float, u); }
DI unsigned asu(float f) { return __builtin_bit_cast(unsigned, f); }
DI float lo(unsigned u) { return asf(u << 16); }
DI float hi(unsigned u) { return asf(u & 0xFFFF0000u); }
DI unsigned pk(float a, float b) { f32x2 f = {a, b}; return __builtin_bit_cast(unsigned, __builtin_convertvector(f, bf16x2)); }
DI float rb(float a) { return lo(pk(a, a)); }
DI unsigned san(unsigned u) { return u < 0xFFFFFFFEu ? u : 0xFFFFFFFEu; }
DI unsigned umax(unsigned a, unsigned b) { return a > b ? a : b; }
DI unsigned mx4(v4u v) { return umax(v.x, v.z); }
DI v4u san4(unsigned a, unsigned b, unsigned c, unsigned d) { return v4u{san(a), b, san(c), d}; }
DI unsigned sel(unsigned m, unsigned a, unsigned b) { return (a & m) | (b & ~m); }
DI void dma(int lds, int voff, rsrc_t rs, int soff, bool nt) {
  if (nt) asm volatile("s_mov_b32 m0, %0\n\ts_nop 0\n\tbuffer_load_dwordx4 %1, %2, %3 offen nt lds" :: "s"(lds), "v"(voff), "s"(rs), "s"(soff) : "memory");
  else asm volatile("s_mov_b32 m0, %0\n\ts_nop 0\n\tbuffer_load_dwordx4 %1, %2, %3 offen lds" :: "s"(lds), "v"(voff), "s"(rs), "s"(soff) : "memory");
}
DI void touch(int lds, int voff, rsrc_t rs) { asm volatile("s_mov_b32 m0, %0\n\ts_nop 0\n\tbuffer_load_dword %1, %2, 0 offen lds" :: "s"(lds), "v"(voff), "s"(rs) : "memory"); }
DI unsigned now32() { return (unsigned)__builtin_amdgcn_s_memrealtime(); }
DI void wait_until(unsigned t) {
  NOUNROLL
  for (int i = 0; i < 4096; i++) if ((int)(now32() - t) >= 0) break;
  FENCE();
}
DI v2u trd(int a) { return __builtin_bit_cast(v2u, __builtin_amdgcn_ds_read_tr16_b64_v4i16((LAS s16x4*)(size_t)(unsigned)a)); }
DI float fmx(float a, float b) { return __builtin_elementwise_maximum(a, b); }
DI f32x4 mfma32(v4u a, v4u b, f32x4 c) {
  return __builtin_amdgcn_mfma_f32_16x16x32_bf16(__builtin_bit_cast(bf16x8, a), __builtin_bit_cast(bf16x8, b), c, 0, 0, 0);
}
DI f32x4 mfma16(v2u a, v2u b, f32x4 c) {
  return __builtin_amdgcn_mfma_f32_16x16x16bf16_1k(__builtin_bit_cast(s16x4, a), __builtin_bit_cast(s16x4, b), c, 0, 0, 0);
}
DI unsigned plo(unsigned a, unsigned b) { return __builtin_amdgcn_perm(b, a, 0x05040100u); }
DI unsigned phi(unsigned a, unsigned b) { return __builtin_amdgcn_perm(b, a, 0x07060302u); }
DI float opq(float v) { asm("" : "+v"(v)); return v; }
template <int CTRL> DI float dpp(float v) { return asf(__builtin_amdgcn_update_dpp(0, (int)asu(v), CTRL, 0xF, 0xF, true)); }
DI float sum_r48(float v) { v += dpp<0x128>(v); v += dpp<0x124>(v); return opq(v); }
DI float sum_x16(float v) { auto r = __builtin_amdgcn_permlane16_swap(asu(v), asu(v), false, false); return asf(r[0]) + asf(r[1]); }
DI float sum_x32(float v) { auto r = __builtin_amdgcn_permlane32_swap(asu(v), asu(v), false, false); return asf(r[0]) + asf(r[1]); }
template <int CTRL> DI unsigned dppu(unsigned v) { return (unsigned)__builtin_amdgcn_update_dpp(0, (int)v, CTRL, 0xF, 0xF, true); }
DI float sum_row(float v) { v += dpp<0x128>(v); v += dpp<0x124>(v); v += dpp<0x122>(v); v += dpp<0x121>(v); return v; }
DI unsigned wave_sum(unsigned cs) {
  cs += dppu<0x128>(cs); cs += dppu<0x124>(cs); cs += dppu<0x122>(cs); cs += dppu<0x121>(cs);
  return (unsigned)(__builtin_amdgcn_readlane(cs, 0) + __builtin_amdgcn_readlane(cs, 16)) + (unsigned)(__builtin_amdgcn_readlane(cs, 32) + __builtin_amdgcn_readlane(cs, 48));
}
DI float max_x16(float v) { auto r = __builtin_amdgcn_permlane16_swap(asu(v), asu(v), false, false); return fmx(asf(r[0]), asf(r[1])); }
DI float max_x32(float v) { auto r = __builtin_amdgcn_permlane32_swap(asu(v), asu(v), false, false); return fmx(asf(r[0]), asf(r[1])); }
#define PIN(x) asm volatile("" : "+v"(x))
#define SCHED() __builtin_amdgcn_sched_barrier(0)
#define BAR_L() asm volatile("s_waitcnt lgkmcnt(0)\n\ts_barrier" ::: "memory")
#define BAR_N() asm volatile("s_barrier" ::: "memory")
#define VMW(n) asm volatile("s_waitcnt vmcnt(" #n ")" ::: "memory")
#define SLEEP(n) do { if ((n) > 0) __builtin_amdgcn_s_sleep(n); } while (0)
template <typename T> DI T lds_ld(int off) { return *(const LAS T*)(size_t)(unsigned)off; }
template <typename T> DI void lds_st(int off, T v) { *(LAS T*)(size_t)(unsigned)off = v; }
DI void slot_set(int off, unsigned tag) { asm volatile("s_waitcnt lgkmcnt(0)" ::: "memory"); *(volatile LAS unsigned*)(size_t)(unsigned)off = tag; }
DI void slot_wait1(int off, unsigned tag) {
  NOUNROLL
  for (int spin = 0; spin < SPIN_CAP * 8; spin++) {
    FENCE();
    if ((unsigned)__builtin_amdgcn_readfirstlane(lds_ld<unsigned>(off)) == tag) break;
  }
  FENCE();
}
template <int NQ> DI void slot_wait(int off, unsigned tag) {
  NOUNROLL
  for (int spin = 0; spin < SPIN_CAP * 8; spin++) {
    FENCE();
    unsigned bad = 0;
#pragma unroll
    for (int i = 0; i < NQ; i++) { const v4u v = lds_ld<v4u>(off + i * 16); bad |= ((v.x ^ tag) | (v.y ^ tag)) | ((v.z ^ tag) | (v.w ^ tag)); }
    if (__builtin_amdgcn_readfirstlane(bad) == 0) break;
  }
  FENCE();
}
const f32x4 Z4 = {0.f, 0.f, 0.f, 0.f};

#define ST(i) do {} while (0)
#define STN(i, v) do {} while (0)
#define ST0() do {} while (0)

#define ARGS_READY() asm volatile("s_load_dwordx16 %0, %4, 0x0\n\ts_load_dwordx4 %1, %4, 0x40\n\ts_load_dwordx2 %2, %4, 0x50\n\ts_load_dwordx2 %3, %4, %5\n\ts_waitcnt lgkmcnt(0)" \
                                  : "=&s"(ka), "=&s"(kb), "=&s"(ws), "=&s"(tb) : "s"(__builtin_amdgcn_kernarg_segment_ptr()), "s"(tk * 8) : "s98", "s99", "memory")
__global__ void __launch_bounds__(512, 2) __attribute__((aligned(4096)))
mla_reglu_decode(const u16*, const u16*, const u16*, const u16*, const u16*, const u16*, const u16*, const u16*, const u16*, u16*, unsigned char*) {
  __shared__ __attribute__((aligned(1024))) unsigned char smem[L_END];
  u64x8 ka;
  u64x2 kb;
  u64 ws;
  asm volatile("s_load_dword s98, %0, 0x0\n\ts_load_dword s99, %0, 0x54" :: "s"(__builtin_amdgcn_kernarg_segment_ptr()) : "s98", "s99");
  SCHED();
  u64 pc0;
  asm volatile("s_getpc_b64 %0" : "=s"(pc0));
  const int tid = threadIdx.x, l = tid & 63, w = __builtin_amdgcn_readfirstlane(tid >> 6), v = w & 3;
  const int n = l & 15, g4 = l >> 4;
  const int bid = blockIdx.x, xg = bid & 7, r = bid >> 3;
  const int lb = (int)(unsigned)(size_t)(LAS unsigned char*)smem;
  if (w == 7) touch(lb + L_SINK, ((bid & ~7) + (l & 7)) * 128 + ((l >> 3) << 24), mk(pc0 & ~127ull, CODE_LINES * 128));
  SCHED();
  const int ab = xg >> 1, as = (xg & 1) * 16 + (r >> 1), ag = r & 1;
  const int apid = (ab * 2 + ag) * 32 + as;
  const int hA = 4 * xg + (r >> 3), dh = r & 1, cqA = (r & 7) >> 1;
  const bool isC = (r & 1) == 0;
  const int idx = r >> 1;
  const int hl = idx >> 2, hC = 4 * xg + hl, cq = idx & 3, cg = idx;
  const int fg = 14 * xg + idx;
  const int rho = 16 * xg + idx;
  v4u sent4 = {SENT, SENT, SENT, SENT};
  struct { unsigned x, y; } tag;
  v4u ones = {0x3F803F80u, 0x3F803F80u, 0x3F803F80u, 0x3F803F80u};
#define TRB(t, rs) (lb + L_C + (8 * g4 + 4 * (t) + (n >> 2)) * (rs) + ((((l & 3) >> 1) ^ ((n & 12) | ((2 * g4 + (t)) & 3))) * 16) + 8 * (l & 1))

  const int half = (l >> 3) & 1, ch = l & 7;
  int o_a = (32 * v + 16 * half + 4 * g4) * 8192 + hA * 256 + dh * 128 + ch * 16, o_b = (l >> 3) * 8192 + ((ch ^ (l >> 3)) * 16), o_x = v * 1024 + l * 16;
  int o_t = (((cqA << 20) & -((4 >> w) & 1)) | (((ab * 32 + as) << 17) & -((8 >> w) & 1)) | ((1 << 21) & -((2 >> w) & 1))) +
            (l << (2 * ((0x1111CCAA >> (4 * w)) & 15))) + ((l >> 1) << 24);
  const int tk = (0x1322 >> (4 * v)) & 15;
  u64 tb;
  PIN(o_a); PIN(o_b); PIN(o_x); PIN(o_t); PIN(sent4);
  ARGS_READY();
  const rsrc_t rws = mk(ws, WS_END), rx = mk(ka[0], 4096);
  if (w >= 4) {
    dma(lb + L_XN + v * 1024, o_x, rx, 0, false);
    asm volatile("s_mov_b32 m0, %0\n\ts_nop 0\n\tbuffer_load_dword %1, %2, 0 offen sc1 lds" :: "s"(lb + L_EV), "v"(o_t), "s"(rws) : "memory");
  } else if (TLBW & 1) touch(lb + L_SINK, o_t, mk(tb, w == 3 ? 16 << 20 : 4 << 20));
  BAR_N();
  ST0();
  ST(14);
  const rsrc_t rc = mk(ka[1], 16 << 20);
  const int gbase = (ab * 4096 + as * 128 + 32 * v) * 1024, lbase = lb + L_C + 32 * v * 1024;
#define CKV(j) dma(lbase + (8 * (((j) & 15) >> 2) + 4 * ((j) >> 4) + ((j) & 3)) * 1024, (l * 16) ^ (((((j) & 3) << 2) | ((2 * (((j) & 15) >> 2) + ((j) >> 4)) & 3)) * 16), rc, gbase + (j) * 1024, NTC)
  v4u wr[16];
  if (w < 4) {
    const rsrc_t rq = mk(ka[2], 4 << 20), rk = mk(ka[3], 4 << 20);
#pragma unroll
    for (int i = 0; i < KAT; i++) wr[i] = ld4<0>(rq, o_a, ((((i >> 2) + cqA) & 3) << 20) + (i & 3) * 8192);
#pragma unroll
    for (int i = 0; i < 4; i++) dma(lb + L_Q + w * 4096 + i * 1024, o_b, rk, (128 * cqA + 32 * w + 8 * i) * 8192 + hA * 256 + dh * 128, false);
    ST(9);
#pragma unroll
    for (int i = KAT; i < 16; i++) wr[i] = ld4<0>(rq, o_a, ((((i >> 2) + cqA) & 3) << 20) + (i & 3) * 8192);
  } else {
    __builtin_amdgcn_s_setprio(3);
    {
      const int s1 = WS_HA + ((xg * 4 + hl) * 4 + cq) * 1024, s2 = WS_HB + (((v & 1) * 32 + xg) * 16 + cg) * 256;
      st4(rws, sent4, (v < 2 ? s1 : s2) + ((ag | (1 >> v)) << 30) + (g4 << (v < 2 ? 8 : 15)) + n * 16);
    }
    __builtin_amdgcn_s_setprio(0);
    {
      VMW(2);
      const v4u xr = lds_ld<v4u>(lb + L_XN + o_x);
      float x8[8];
#pragma unroll
      for (int j = 0; j < 4; j++) { x8[2 * j] = lo(xr[j]); x8[2 * j + 1] = hi(xr[j]); }
      const float mean = sum_x32(sum_x16(sum_row(((x8[0] + x8[1]) + (x8[2] + x8[3])) + ((x8[4] + x8[5]) + (x8[6] + x8[7]))))) * (1.0f / 512.0f);
      float q = 0.f;
#pragma unroll
      for (int j = 0; j < 8; j++) q += (x8[j] - mean) * (x8[j] - mean);
      const float rs = __builtin_amdgcn_rcpf(rb(__builtin_amdgcn_sqrtf(rb(rb(sum_x32(sum_x16(sum_row(q))) * (1.0f / 512.0f)) + 1e-5f)))), mb = rb(mean);
      v4u o;
#pragma unroll
      for (int j = 0; j < 4; j++) { const unsigned d = pk(x8[2 * j] - mb, x8[2 * j + 1] - mb); o[j] = pk(lo(d) * rs, hi(d) * rs); }
      lds_st<v4u>(lb + L_XN + o_x, o);
    }
    if (l < 6) lds_st<unsigned>(lb + L_CNT + (v * 6 + l) * 4, 0u);
    VMW(0);
  }
  ST(1);
  BAR_L();
  {
    const v2u t = lds_ld<v2u>(lb + L_EV);
    const u64 e = (((u64)(unsigned)__builtin_amdgcn_readfirstlane(t.y) << 32) | (unsigned)__builtin_amdgcn_readfirstlane(t.x)) + 1;
    tag.x = (unsigned)e;
    tag.y = (unsigned)(e >> 32);
  }
  unsigned tz;
  v2u hi_;
  int c_vk, c_vo, c_cx, c_wg, c_ww, c_vm, c_bx, c_ha, c_t0, c_t1;

  if (w >= 4) {
    v4u cf[32];
    int qa_[4], cl[8];
    int a_mx = lb + L_MX + n * 16, a_pw = lb + L_P + v * 1024 + l * 16;
    {
      const rsrc_t rg = mk((v & 1) ? ka[7] : ka[6], ZGU == 1 && !isC ? 512 * 3584 : 0), ro = mk(ka[5], isC ? 0 : 4 << 20);
      const int tg = (idx * 32 + (v >> 1) * 16 + (n >> 2)) * 3584 + ((448 * xg) & ~127) + (l & 3) * 128 + (g4 << 28);
      const int to = ((xg * 16 + idx) * 4 + v) * 8192 + n * 128 + (g4 << 28);
      if (TLBW & 2) {
        const int k = min(r >> 2, 6), e = (r & 3) * 4 + v;
        u64 tb2;
        asm volatile("s_load_dwordx2 %0, %1, %2\n\ts_waitcnt lgkmcnt(0)" : "=&s"(tb2) : "s"(__builtin_amdgcn_kernarg_segment_ptr()), "s"(32 + 8 * k) : "memory");
        const int m6 = -((0x40 >> k) & 1);
        touch(lb + L_SINK, ((WS_OP + 16384) & m6) + e * ((1 << (k < 2 || k == 6 ? 18 : 17)) + (2048 & m6)) + (l << 24), mk(tb2, max((int)((0x17000707071010ull >> (8 * k)) & 255) << 18, 4096)));
      }
#pragma unroll
      for (int j = 0; j < 32; j++) {
        if (j >= KTH) asm volatile("s_waitcnt vmcnt(%0)" :: "i"(KTH - 1) : "memory");
        CKV(j);
        if (j >= 8 && j < 16 && (ZGU == 1 || (j & 1))) {
          asm volatile("s_waitcnt vmcnt(%0)" :: "i"(KTH - 2) : "memory");
          if (j & 1) touch(lb + L_SINK, to + ((j - 8) >> 1) * 2048, ro); else touch(lb + L_SINK, tg + ((j - 8) >> 1) * 4 * 3584, rg);
        }
        if (j == KTH + 3) {
          const int qb = lb + L_Q + n * 1024 + ((g4 ^ n) * 16);
#pragma unroll
          for (int i = 0; i < 4; i++) { qa_[i] = qb ^ (i * 64); PIN(qa_[i]); }
#pragma unroll
          for (int i = 0; i < 8; i++) {
            const int t = i & 1, key = 32 * v + 8 * (n >> 2) + 4 * t + (n & 3), f = ((n & 3) << 2) | ((2 * (n >> 2) + t) & 3);
            cl[i] = (lb + L_C + key * 1024 + ((g4 ^ f) * 16)) ^ ((i >> 1) * 64);
            PIN(cl[i]);
          }
        }
        if (j == KTH + 5) { PIN(a_mx); PIN(a_pw); }
        if (j == KTH + 11) {
          const int pb = (v * 2 + (hC >> 4)) * 32;
          c_vk = WS_CK + (((hC * 4 + cq) * 4 + v) * 32 + (l & 31)) * 16;
          c_vo = WS_OP + (pb + 8 * g4) * OPS + (hC & 15) * 1024 + cq * 256 + n * 16;
          c_cx = lb + L_CX + n * 64 + v * 16 + (g4 & 1) * 8 + (g4 >> 1) * 1024;
          c_wg = lb + L_WG + v * 128 + g4 * 16;
          c_ww = lb + L_WG + v * 128 + l * 2;
          PIN(c_vk); PIN(c_vo); PIN(c_cx); PIN(c_wg); PIN(c_ww);
        }
        if (j == KTH + 9) {
          c_vm = WS_ML + ((v * 2 + (hC >> 4)) * 32 + (l & 31)) * 256 + (ag << 30);
          c_bx = lb + L_CX + g4 * 64 + (n & 3) * 16;
          c_ha = WS_HA + ((xg * 4 + hl) * 4 + cq) * 1024 + v * 256 + g4 * 64 + (n & 3) * 16 + ((n >> 2) << 28);
          c_t0 = TRB(0, 256) ^ (v * 64);
          c_t1 = TRB(1, 256) ^ (v * 64);
          PIN(c_vm); PIN(c_bx); PIN(c_ha); PIN(c_t0); PIN(c_t1);
        }
        if (j == 15 + KTH) {
#pragma unroll
          for (int i = 0; i < 16; i++) cf[i] = lds_ld<v4u>(cl[2 * (i & 3)] + (i >> 2) * 256);
          SCHED();
        }
      }
    }
    ST(2);
    __builtin_amdgcn_s_setprio(2);
    BAR_N();
    ST(4);
    v4u qf[16];
#pragma unroll
    for (int i = 0; i < 16; i++) qf[i] = lds_ld<v4u>(qa_[i & 3] + (i >> 2) * 256);
    SCHED();
    f32x4 s0 = Z4, s1 = Z4;
#pragma unroll
    for (int i = 0; i < SPLIT_S; i++) s0 = mfma32(cf[i], qf[i], s0);
    SCHED();
    VMW(0);
    ST(6);
    *(volatile LAS unsigned*)(size_t)(unsigned)(lb + L_CNT + 16 + v * 4) = 1u;
#pragma unroll
    for (int i = 0; i < 16; i++) cf[16 + i] = lds_ld<v4u>(cl[2 * (i & 3) + 1] + (i >> 2) * 256);
    SCHED();
    ST(3);
#pragma unroll
    for (int i = SPLIT_S; i < 16; i++) s0 = mfma32(cf[i], qf[i], s0);
#pragma unroll
    for (int i = 0; i < 16; i++) s1 = mfma32(cf[16 + i], qf[i], s1);
    float m = fmx(fmx(fmx(s0[0], s0[1]), fmx(s0[2], s0[3])), fmx(fmx(s1[0], s1[1]), fmx(s1[2], s1[3])));
    m = max_x32(max_x16(m));
    lds_st<float>(a_mx + v * 4, m);
    BAR_L();
    ST(5);
    {
      const f32x4 mm = lds_ld<f32x4>(a_mx);
      m = fmx(fmx(mm[0], mm[1]), fmx(mm[2], mm[3]));
      float p[8];
#pragma unroll
      for (int i = 0; i < 4; i++) { p[i] = __builtin_amdgcn_exp2f(s0[i] - m); p[4 + i] = __builtin_amdgcn_exp2f(s1[i] - m); }
      const v4u o = {pk(p[0], p[1]), pk(p[2], p[3]), pk(p[4], p[5]), pk(p[6], p[7])};
      lds_st<v4u>(a_pw, o);
    }
    hi_ = ld2<SC1>(rws, c_vm);
    BAR_L();
    ST(7);
    ST(8);
  } else {
    int a_kr = lb + L_Q + w * 4096 + (8 * (n >> 2) + (n & 3)) * 128 + ((g4 ^ (n & 3)) * 16);
    PIN(a_kr);
    int a_qpw = lb + L_QP + half * 2048 + ch * 256, a_qpr = lb + L_QP + g4 * 256 + ((n ^ g4) * 16);
    int o_qa = WS_QA + ((((n & 3) * 2 + (hA >> 4)) * 2 + dh) * 16 + (hA & 15)) * 1024 + (128 * cqA + 32 * w + 8 * g4) * 2 + ((n >> 2) << 28);
    int o_qk = WS_QK + ((((hA >> 4) * 4 + (hA & 3)) * 4 + ((hA >> 2) & 3)) * 8 + (r & 7)) * 64 + (l & 3) * 16 + w * 4 + ((l >> 2) << 26);
    int ax = lb + L_XN + (n & 3) * 1024 + (32 * w + 16 * ((n >> 2) & 1) + 4 * g4) * 2;
    PIN(a_qpw); PIN(a_qpr); PIN(o_qa); PIN(o_qk); PIN(ax);
    v4u xa[2];
#pragma unroll
    for (int i = 0; i < 4; i++) { const v2u t = lds_ld<v2u>(ax + ((i + cqA) & 3) * 256); xa[i >> 1][(i & 1) * 2] = t.x; xa[i >> 1][(i & 1) * 2 + 1] = t.y; }
    SCHED();
    {
      f32x4 acc[8];
#pragma unroll
      for (int u = 0; u < 2; u++) {
#pragma unroll
        for (int c = 0; c < 8; c++) {
          v4u f;
#pragma unroll
          for (int jp = 0; jp < 4; jp++) {
            const unsigned a = wr[u * 8 + 2 * jp][c >> 1], b = wr[u * 8 + 2 * jp + 1][c >> 1];
            f[jp] = (c & 1) ? phi(a, b) : plo(a, b);
          }
          acc[c] = mfma32(xa[u], f, u ? acc[c] : Z4);
        }
        SCHED();
      }
      if (g4 == half) {
#pragma unroll
        for (int b = 0; b < 4; b++) {
          const v4u o = {pk(acc[0][b], acc[1][b]), pk(acc[2][b], acc[3][b]), pk(acc[4][b], acc[5][b]), pk(acc[6][b], acc[7][b])};
          lds_st<v4u>(a_qpw + (((4 * w + b) ^ ch) * 16), o);
        }
      }
    }
    ST(2);
    VMW(0);
    v4u kr[4];
#pragma unroll
    for (int i = 0; i < 4; i++) kr[i] = lds_ld<v4u>((a_kr ^ (((i & 1) ^ (i >> 1)) * 64)) + (i >> 1) * 512);
    slot_set(lb + L_CNT + w * 4, 1u);
    slot_wait<1>(lb + L_CNT, 1u);
    {
      v4u bq[4];
#pragma unroll
      for (int i = 0; i < 4; i++) bq[i] = lds_ld<v4u>((a_qpr ^ ((i & 1) * 64)) + (i >> 1) * 2048 + (i & 1) * 1024);
      f32x4 d0 = Z4, d1 = Z4;
#pragma unroll
      for (int i = 0; i < 4; i++) { d0 = mfma32(kr[i & 1], bq[i], d0); d1 = mfma32(kr[2 + (i & 1)], bq[i], d1); }
      const float sc = 0.08838834764831845f * 1.4426950408889634f;
#pragma unroll
      for (int i = 0; i < 4; i++) { d0[i] = sum_r48(d0[i]) * sc; d1[i] = sum_r48(d1[i]) * sc; }
      const v4u o = {pk(d0[0], d0[1]) + tag.x, pk(d0[2], d0[3]), pk(d1[0], d1[1]) + tag.x, pk(d1[2], d1[3])};
      st4(rws, o, o_qa);
      unsigned cs = ((o.x + o.y) + (o.z + o.w)) + (tag.x << 10);
      { auto t = __builtin_amdgcn_permlane16_swap(cs, cs, false, false); cs = t[0] + t[1]; }
      { auto t = __builtin_amdgcn_permlane32_swap(cs, cs, false, false); cs = t[0] + t[1]; }
      __builtin_amdgcn_raw_buffer_store_b32(cs, rws, o_qk, 0, SC1);
    }
    const unsigned t_pub = now32();
    ST(3);
    v4u wv[8];
    const rsrc_t rv = mk(ka[4], 4 << 20);
    const int o_wv = (128 * cq + 32 * w + g4) * 8192 + hC * 256 + n * 16 + (((ABL & 4) ? 1 : ag) << 30);
    int ta[16];
#pragma unroll
    for (int i = 0; i < 16; i++) { ta[i] = (TRB(i & 1, 1024) ^ ((i >> 1) * 32)) + w * 256; PIN(ta[i]); }
    int o_op = WS_OP + apid * OPS + n * 1024 + w * 256 + g4 * 16, a_p = lb + L_P + l * 16;
    int o_ck = WS_CK + ((((ag * 16 + n) * 4 + w) * 4 + ab) * 32 + as) * 16 + (g4 << 28), a_mx = lb + L_MX + n * 16;
    PIN(a_mx); PIN(ones);
    int o_hs = WS_ML + apid * 256 + (tid << 23);
    int a_wv = lb + L_C + w * 32768 + g4 * 256 + ((n ^ (g4 << 2)) * 16);
    PIN(o_op); PIN(a_p); PIN(o_ck); PIN(o_hs); PIN(a_wv);
    int vq = WS_QA + (ab * 2 + ag) * 32768 + w * 1024 + l * 16, vk = WS_QK + ((ag * 4 + w) * 32 + (l >> 1)) * 64 + ab * 16 + (l & 1) * 8;
    int aq = lb + L_Q + w * 1024 + g4 * 256 + ((n ^ w) * 16);
    int o_wo = ((4 * xg + w) * 128 + 16 * ((l >> 4) & 1) + 4 * (l >> 5) + ((l >> 2) & 3)) * 1024 + cg * 64 + (l & 3) * 16 + (ag << 30);
    const rsrc_t ro = mk(ka[5], 4 << 20);
    v4u wo_[8];
    int o_gu = (idx * 32 + (w >> 1) * 16 + (l >> 2)) * 3584 + ((448 * xg) & ~127) + (l & 3) * 128;
    PIN(o_gu);
    PIN(vq); PIN(vk); PIN(aq); PIN(o_wo);
#pragma unroll
    for (int i = 0; i < 8; i++) { wv[i] = ld4<0>(rv, o_wv, (16 * (i & 1) + 4 * (i >> 1)) * 8192); SLEEP(WV_S); }
    wait_until(t_pub + T_H1);
    ST(15);
    {
      int spin = 0;
      NOUNROLL
      for (; spin < ((ABL & 2) ? 0 : SPIN_CAP); spin++) {
        FENCE();
        const v2u k = ld2<SC1>(rws, vk);
        v4u qa[8];
#pragma unroll
        for (int i = 0; i < 8; i++) qa[i] = ld4<SC1>(rws, vq, (i >> 1) * 4096 + (i & 1) * 16384);
        SCHED();
        unsigned cs = (tag.x << 13) - (k.x + k.y);
        SCHED();
#pragma unroll
        for (int i = 0; i < 4; i++) {
          v4u a = qa[2 * i], b = qa[2 * i + 1], o;
          cs += ((a.x + a.y) + (a.z + a.w)) + ((b.x + b.y) + (b.z + b.w));
          a.x -= tag.x; a.z -= tag.x; b.x -= tag.x; b.z -= tag.x;
#pragma unroll
          for (int j = 0; j < 4; j++) o[j] = pk(lo(a[j]) + lo(b[j]), hi(a[j]) + hi(b[j]));
          lds_st<v4u>((aq ^ (i * 64)) + i * 4096, o);
          if (i < 3) SCHED();
        }
        if (wave_sum(cs) == 0u) break;
      }
      STN(19, spin);
    }
    ST(5);
    BAR_L();
    ST(6);
    v4u cf[32];
#define RDT(t, k0) _Pragma("unroll") for (int ct = 0; ct < 8; ct++) _Pragma("unroll") for (int ks = k0; ks < k0 + 2; ks++) { \
      const v2u v_ = trd(ta[ct * 2 + t] + ks * 32768); cf[ct * 4 + ks][2 * t] = v_.x; cf[ct * 4 + ks][2 * t + 1] = v_.y; }
    RDT(0, 0) RDT(0, 2)
    SCHED();
#pragma unroll
    for (int i = 0; i < WO_N; i++) wo_[i] = ld4<0>(ro, o_wo, (32 * (i >> 1) + 8 * (i & 1)) * 1024);
    SCHED();
    slot_wait<1>(lb + L_CNT + 16, 1u);
    st2(rws, v2u{now32(), tag.x}, o_hs);
#pragma unroll
    for (int i = WO_N; i < 8; i++) wo_[i] = ld4<0>(ro, o_wo, (32 * (i >> 1) + 8 * (i & 1)) * 1024);
    if (ZGU == 2) touch(lb + L_SINK, o_gu, mk((w & 1) ? ka[7] : ka[6], isC ? 0 : 512 * 3584));
    RDT(1, 0)
    SCHED();
    ST(4);
    BAR_N();
    RDT(1, 2)
    SCHED();
    unsigned ms;
    { const f32x4 mm = lds_ld<f32x4>(a_mx); ms = asu(fmx(fmx(mm[0], mm[1]), fmx(mm[2], mm[3]))) + tag.x; PIN(ms); }
    BAR_L();
    __builtin_amdgcn_s_setprio(3);
    ST(7);
    {
      v4u pf[4];
#pragma unroll
      for (int ks = 0; ks < 4; ks++) pf[ks] = lds_ld<v4u>(a_p + ks * 1024);
      SCHED();
      f32x4 d[8], ls = Z4;
      unsigned cs = tag.x << 10;
#pragma unroll
      for (int a = 0; a < 5; a++) {
        if (a < 4) {
#pragma unroll
          for (int ks = 0; ks < 2; ks++) {
            d[2 * a] = mfma32(cf[(2 * a) * 4 + ks], pf[ks], ks ? d[2 * a] : Z4);
            d[2 * a + 1] = mfma32(cf[(2 * a + 1) * 4 + ks], pf[ks], ks ? d[2 * a + 1] : Z4);
          }
        }
        if (a == 4) {
#pragma unroll
          for (int ks = 0; ks < 4; ks++) ls = mfma32(ones, pf[ks], ls);
        }
        if (a > 0) {
          const f32x4 d0 = d[2 * a - 2], d1 = d[2 * a - 1];
          const v4u o = {pk(d0[0], d0[1]) + tag.x, pk(d0[2], d0[3]), pk(d1[0], d1[1]) + tag.x, pk(d1[2], d1[3])};
          st4(rws, o, o_op, (a - 1) * 64);
          cs += (o.x + o.y) + (o.z + o.w);
        }
        if (a < 4) {
#pragma unroll
          for (int ks = 2; ks < 4; ks++) {
            d[2 * a] = mfma32(cf[(2 * a) * 4 + ks], pf[ks], d[2 * a]);
            d[2 * a + 1] = mfma32(cf[(2 * a + 1) * 4 + ks], pf[ks], d[2 * a + 1]);
          }
        }
        SCHED();
      }
      { auto t = __builtin_amdgcn_permlane16_swap(cs, cs, false, false); cs = t[0] + t[1]; }
      { auto t = __builtin_amdgcn_permlane32_swap(cs, cs, false, false); cs = t[0] + t[1]; }
      st4(rws, v4u{ms, asu(ls[0]), cs + ms + asu(ls[0]), 0u}, o_ck);
    }
    __builtin_amdgcn_s_setprio(0);
    ST(8);
    if (isC) {
#pragma unroll
      for (int i = 0; i < 8; i++) lds_st<v4u>((a_wv ^ ((i & 3) * 16)) + i * 1024, wv[i]);
    }
    slot_set(lb + L_CNT + 32 + w * 4, 1u);
    if (isC) {
#pragma unroll
      for (int i = 0; i < 8; i++) lds_st<v4u>(lb + L_WO + w * 32768 + l * 16 + i * 1024, wo_[i]);
    }
  }
  if (isC) {
    v4u af[8];
    if (w >= 4) {
      slot_wait<1>(lb + L_CNT + 32, 1u);
#pragma unroll
      for (int ks = 0; ks < 4; ks++)
#pragma unroll
        for (int e = 0; e < 2; e++)
#pragma unroll
          for (int t = 0; t < 2; t++) {
            const v2u v_ = trd(((t ? c_t1 : c_t0) ^ (e * 32)) + ks * 32768);
            af[4 * e + ks][2 * t] = v_.x;
            af[4 * e + ks][2 * t + 1] = v_.y;
          }
      unsigned lo32 = l < 32 ? ~0u : 0u;
      int aw[8];
#pragma unroll
      for (int i = 0; i < 8; i++) { aw[i] = c_wg + (n == i ? 0 : 64); PIN(aw[i]); }
      PIN(lo32);
      SCHED();
      {
        int spin = 0;
        NOUNROLL
        for (; spin < SPIN_CAP; spin++) {
          if (!__any(hi_.y != tag.x)) break;
          SLEEP(DL_MR);
          FENCE();
          hi_ = ld2<SC1>(rws, c_vm);
        }
        STN(20, spin);
        const unsigned base = now32() - 0x100000u;
        unsigned d = hi_.x - base;
        d = umax(d, dppu<0x128>(d)); d = umax(d, dppu<0x124>(d)); d = umax(d, dppu<0x122>(d)); d = umax(d, dppu<0x121>(d));
        tz = base + umax(__builtin_amdgcn_readlane(d, 0), __builtin_amdgcn_readlane(d, 16));
      }
      ST(13);
      wait_until(tz + T_H2);
      ST(9);
      int spin = 0;
      NOUNROLL
      for (; spin < SPIN_CAP; spin++) {
        FENCE();
        v4u ml = ld4<SC1>(rws, c_vk);
        v4u op[8];
#pragma unroll
        for (int i = 0; i < 8; i++) op[i] = ld4<SC1>(rws, c_vo, i * OPS);
        SCHED();
        unsigned cs = (tag.x << 11) + (((ml.x + ml.y) - ml.z) & lo32);
        ml.x -= tag.x;
        float M = asf(ml.x);
        M = fmx(M, dpp<0x128>(M)); M = fmx(M, dpp<0x124>(M)); M = fmx(M, dpp<0x122>(M)); M = fmx(M, dpp<0x121>(M));
        M = max_x16(M);
        const float we = __builtin_amdgcn_exp2f(asf(ml.x) - M);
        const unsigned wb = pk(we, we);
        lds_st<u16>(c_ww, (u16)(wb & lo32));
        FENCE();
        v4u wa[8];
#pragma unroll
        for (int i = 0; i < 8; i++) wa[i] = lds_ld<v4u>(aw[i]);
        float rden = __builtin_amdgcn_rcpf(sum_x16(sum_row(hi(wb) * asf(ml.y))));
        PIN(rden);
        v4u bf[8];
        SCHED();
#pragma unroll
        for (int i = 0; i < 4; i++) {
          v4u a = op[2 * i], b = op[2 * i + 1];
          cs += ((a.x + a.y) + (a.z + a.w)) + ((b.x + b.y) + (b.z + b.w));
          a.x -= tag.x; a.z -= tag.x; b.x -= tag.x; b.z -= tag.x;
#pragma unroll
          for (int j = 0; j < 4; j++) { bf[2 * j][i] = plo(a[j], b[j]); bf[2 * j + 1][i] = phi(a[j], b[j]); }
          if (i < 3) SCHED();
        }
        f32x4 acc = Z4;
#pragma unroll
        for (int i = 0; i < 8; i++) acc = mfma32(wa[i], bf[i], acc);
        cs += dppu<0x128>(cs); cs += dppu<0x124>(cs); cs += dppu<0x122>(cs); cs += dppu<0x121>(cs);
        lds_st<v2u>(c_cx, v2u{pk(acc[0] * rden, acc[1] * rden), pk(acc[2] * rden, acc[3] * rden)});
        const unsigned tot = (unsigned)(__builtin_amdgcn_readlane(cs, 0) + __builtin_amdgcn_readlane(cs, 16)) + (unsigned)(__builtin_amdgcn_readlane(cs, 32) + __builtin_amdgcn_readlane(cs, 48));
        if (tot == 0u) break;
        SLEEP(DL_RT);
      }
      STN(21, spin);
      ST(10);
      BAR_L();
      v4u bx[4];
#pragma unroll
      for (int ks = 0; ks < 4; ks++) bx[ks] = lds_ld<v4u>(c_bx + ks * 256);
      f32x4 d0 = Z4, d1 = Z4;
#pragma unroll
      for (int ks = 0; ks < 4; ks++) { d0 = mfma32(af[ks], bx[ks], d0); d1 = mfma32(af[4 + ks], bx[ks], d1); }
      const v4u o = san4(pk(d0[0], d0[1]), pk(d0[2], d0[3]), pk(d1[0], d1[1]), pk(d1[2], d1[3]));
      st4(rws, o, c_ha);
      __builtin_amdgcn_s_setprio(0);
      ST(11);
      const v4u xq = ld4<0>(rx, (l & 3) * 1024 + (128 * v + 8 * (l >> 2)) * 2);
      int vh = WS_HB + (xg * 8 * 16 + 4 * v) * 256 + l * 16;
      PIN(vh);
      BAR_L();
      SLEEP(DL_HV);
      NOUNROLL
      for (int sp = 0; sp < SPIN_CAP; sp++) {
        FENCE();
        const v2u f0 = ld2<SC1>(rws, WS_FLG2 + (v * 28 + min(l, 27)) * 32);
        if (!__any(((f0.x ^ tag.x) | (f0.y ^ tag.y)) != 0u)) break;
        __builtin_amdgcn_s_sleep(4);
      }
      float h[8];
      spin = 0;
      NOUNROLL
      for (; spin < SPIN_CAP; spin++) {
        FENCE();
        v4u hp[8];
#pragma unroll
        for (int i = 0; i < 8; i++) hp[i] = ld4<SC1>(rws, vh, i * 4096);
        unsigned m = 0;
#pragma unroll
        for (int j = 0; j < 4; j++) { h[2 * j] = lo(xq[j]); h[2 * j + 1] = hi(xq[j]); }
#pragma unroll
        for (int i = 0; i < 8; i++) {
#pragma unroll
          for (int j = 0; j < 4; j++) { h[2 * j] += lo(hp[i][j]); h[2 * j + 1] += hi(hp[i][j]); }
          m = umax(m, mx4(hp[i]));
        }
        if (!__any(m == SENT)) break;
        SLEEP(DL_RT);
      }
      STN(22, spin);
      ST(12);
      const float mw = sum_x32(sum_x16(sum_r48(((h[0] + h[1]) + (h[2] + h[3])) + ((h[4] + h[5]) + (h[6] + h[7]))))) * (1.0f / 128.0f);
      float m2 = 0.f;
#pragma unroll
      for (int j = 0; j < 8; j++) m2 += (h[j] - mw) * (h[j] - mw);
      m2 = sum_x32(sum_x16(sum_r48(m2)));
      if (l < 4) lds_st<f32x2>(lb + L_VS + l * 32 + v * 8, f32x2{mw, m2});
      if (16 * v + (l >> 2) == (rho >> 1))
        lds_st<f32x4>(lb + L_HO + (l & 3) * 16, (rho & 1) ? f32x4{h[4], h[5], h[6], h[7]} : f32x4{h[0], h[1], h[2], h[3]});
      BAR_L();
    } else {
      int a_wr = lb + L_WO + w * 32768 + (8 * g4 + (n >> 2)) * 64 + (l & 3) * 16;
      int vo = WS_HA + ((xg * 4 + w) * 4 + (n >> 2)) * 1024 + g4 * 64 + (n & 3) * 16;
      int a_r2 = lb + L_R2 + w * 512 + (g4 * 4 + (n & 3)) * 32 + (n < 4 ? 0 : 16), a_r3 = lb + L_R2 + n * 32;
      int o_hb = WS_HB + ((((w & 1) * 4 + g4) * 8 + xg) * 16 + cg) * 256 + n * 16;
      unsigned msk = n < 4 ? ~0u : 0u;
      PIN(vo); PIN(a_r2); PIN(a_r3); PIN(o_hb); PIN(msk); PIN(a_wr);
      asm volatile("s_waitcnt lgkmcnt(0)" ::: "memory");
#pragma unroll
      for (int ks = 0; ks < 4; ks++)
#pragma unroll
        for (int e = 0; e < 2; e++)
#pragma unroll
          for (int t = 0; t < 2; t++) {
            const v2u v_ = trd(a_wr + ks * 2048 + t * 256 + e * 8);
            af[4 * e + ks][2 * t] = v_.x;
            af[4 * e + ks][2 * t + 1] = v_.y;
          }
#pragma unroll
      for (int i = 0; i < 8; i++) PIN(af[i]);
      int spin;
      BAR_L();
      tz = now32();
      if (w == 3 && (r & 6) == 0) st2(rws, v2u{tz, tag.x}, WS_ML + apid * 256 + 128 + (l << 24));
      ST(11);
      wait_until(tz + D_HA);
      f32x4 d0, d1;
      spin = 0;
      NOUNROLL
      for (; spin < SPIN_CAP; spin++) {
        FENCE();
        v4u bx[4];
#pragma unroll
        for (int i = 0; i < 4; i++) bx[i] = ld4<SC1>(rws, vo, i * 256);
        SCHED();
        unsigned m = 0;
#pragma unroll
        for (int ks = 0; ks < 4; ks++) { d0 = mfma32(af[ks], bx[ks], ks ? d0 : Z4); d1 = mfma32(af[4 + ks], bx[ks], ks ? d1 : Z4); m = umax(m, mx4(bx[ks])); SCHED(); }
        if (!__any(m == SENT)) break;
        SLEEP(DL_RT);
      }
      STN(22, spin);
      ST(12);
#pragma unroll
      for (int i = 0; i < 4; i++) { d0[i] = sum_r48(d0[i]); d1[i] = sum_r48(d1[i]); }
      lds_st<v4u>(a_r2, v4u{sel(msk, asu(d0[0]), asu(d1[0])), sel(msk, asu(d0[1]), asu(d1[1])), sel(msk, asu(d0[2]), asu(d1[2])), sel(msk, asu(d0[3]), asu(d1[3]))});
      BAR_L();
      if (w < 2) {
        f32x4 e[8];
#pragma unroll
        for (int i = 0; i < 8; i++) e[i] = lds_ld<f32x4>(a_r3 + (i >> 1) * 512 + (i & 1) * 16);
        const f32x4 e0 = (e[0] + e[2]) + (e[4] + e[6]), e1 = (e[1] + e[3]) + (e[5] + e[7]);
        const v4u o = san4(pk(e0[0], e0[1]), pk(e0[2], e0[3]), pk(e1[0], e1[1]), pk(e1[2], e1[3]));
        st4(rws, o, o_hb);
        ST(13);
      }
      int vf = WS_FO + (l >> 2) * 8192 + rho * 64 + (l & 3) * 16;
      int oo = ((n & 3) * 512 + 4 * rho + g4) * 2 + ((n >> 2) << 28);
      PIN(vf); PIN(oo);
      BAR_L();
      if (w == 2) {
        const f32x4 va = lds_ld<f32x4>(lb + L_VS + (l & 3) * 32), vb = lds_ld<f32x4>(lb + L_VS + (l & 3) * 32 + 16);
        float hv = lds_ld<float>(lb + L_HO + (l & 3) * 16 + g4 * 4);
        const float mean = ((va[0] + va[2]) + (vb[0] + vb[2])) * 0.25f;
        const float dv = ((va[0] - mean) * (va[0] - mean) + (va[2] - mean) * (va[2] - mean)) + ((vb[0] - mean) * (vb[0] - mean) + (vb[2] - mean) * (vb[2] - mean));
        float rs2 = __builtin_amdgcn_rcpf((((va[1] + va[3]) + (vb[1] + vb[3])) + 128.0f * dv) * (1.0f / 512.0f) + 1e-5f);
        PIN(rs2); PIN(hv);
        ST(15);
        SLEEP(DL_T4);
        f32x4 a;
        spin = 0;
        NOUNROLL
        for (; spin < SPIN_CAP; spin++) {
          FENCE();
          v4u fo[7];
#pragma unroll
          for (int i = 0; i < 7; i++) fo[i] = ld4<SC1>(rws, vf, i * 16 * 8192);
          SCHED();
          unsigned m = 0;
#pragma unroll
          for (int i = 0; i < 7; i++) { a = i ? a + __builtin_bit_cast(f32x4, fo[i]) : __builtin_bit_cast(f32x4, fo[i]); m = umax(m, mx4(fo[i])); SCHED(); }
          if (!__any(m == SENT)) break;
        SLEEP(DL_RT);
        }
        STN(23, spin);
        ST(16);
        {
          auto t0 = __builtin_amdgcn_permlane32_swap(asu(a[0]), asu(a[2]), false, false);
          auto t1 = __builtin_amdgcn_permlane32_swap(asu(a[1]), asu(a[3]), false, false);
          auto t2 = __builtin_amdgcn_permlane16_swap(asu(opq(asf(t0[0]) + asf(t0[1]))), asu(opq(asf(t1[0]) + asf(t1[1]))), false, false);
          const float y = hv + rs2 * sum_r48(asf(t2[0]) + asf(t2[1]));
          __builtin_amdgcn_raw_buffer_store_b16((short)pk(y, y), mk(kb[1], 4096), oo, 0, 0);
        }
        if (rho == 0) st2(rws, v2u{tag.x, tag.y}, WS_E + (l << 24));
        ST(17);
      }
    }
  } else {
    if (w >= 4) {
      __builtin_amdgcn_s_setprio(0);
      int t2 = (idx * 32 + (v >> 1) * 16 + (l >> 2)) * 3584 + ((448 * xg) & ~127) + (l & 3) * 128;
      PIN(t2);
      slot_wait<1>(lb + L_CNT + 32, 1u);
      SLEEP(DL_DW);
      ST(9);
      if (!ZGU) touch(lb + L_SINK, t2, mk((v & 1) ? ka[7] : ka[6], 512 * 3584));
    }
   if (idx < 14) {
    if (w >= 4) {
      const rsrc_t rg = mk((v & 1) ? ka[7] : ka[6], 512 * 3584);
      int vg = (l >> 1) * 3584 + (l & 1) * 16;
      PIN(vg);
      SLEEP(DL_PF);
#pragma unroll
      for (int i = 0; i < 8; i++) {
        const int ks = (((i >> 1) + fg) & 3) * 4 + (i & 1) * 2 + (v >> 1);
        dma(lb + L_FG + (v & 1) * 16384 + ks * 1024, vg, rg, ks * 32 * 3584 + fg * 32, false);
      }
      {
        const rsrc_t rd = mk(kb[0], 1792 * 1024);
#pragma unroll
        for (int j = 0; j < 4; j++) dma(lb + L_FD + (4 * v + j) * 1056, l * 16, rd, (16 * fg + 4 * v + j) * 1024, false);
      }
      st4(rws, sent4, WS_FO + fg * 8192 + v * 2048 + l * 16);
      st4(rws, sent4, WS_FO + fg * 8192 + v * 2048 + l * 16, 1024);
      ST(10);
      VMW(0);
      ST(11);
      BAR_N();
      if (v == 0) st2(rws, v2u{tag.x, tag.y}, WS_FLG2 + fg * 32 + (l << 24));
      BAR_N();
      BAR_N();
    } else {
      v4u af[8], df[4], xq[4];
#pragma unroll
      for (int kk = 0; kk < 4; kk++) xq[kk] = ld4<0>(rx, (n & 3) * 1024 + (32 * w + 8 * g4) * 2 + ((n >> 2) << 28), kk * 256);
      int vo = WS_HB + ((xg * 8 + (n >> 2)) * 16 + w) * 256 + g4 * 64 + (n & 3) * 16;
      int a_gw = lb + L_GU + (w * 4 + (n & 3)) * 128 + g4 * 32 + (n < 4 ? 0 : 16);
      int a_gr = lb + L_GU + n * 128 + g4 * 32;
      int o_fo = WS_FO + fg * 8192 + w * 2048 + (n >> 2) * 256 + g4 * 64 + (n & 3) * 16;
      int o_hi = WS_ML + (((l >> 1) & 3) * 64 + (l & 1) * 16 + ((l >> 3) & 3) * 4) * 256 + 128;
      int a_fg = lb + L_FG + w * 1024 + (8 * g4 + (n >> 2)) * 32 + (l & 3) * 8, a_fd = lb + L_FD + (4 * g4 + (n >> 2)) * 1056 + w * 256 + (l & 3) * 8;
      unsigned mq[4];
#pragma unroll
      for (int i = 0; i < 4; i++) { mq[i] = (n >> 2) == i ? ~0u : 0u; PIN(mq[i]); }
      PIN(ones); PIN(vo); PIN(a_gw); PIN(a_gr); PIN(o_fo); PIN(o_hi); PIN(a_fg); PIN(a_fd);
      BAR_N();
#pragma unroll
      for (int e = 0; e < 2; e++)
#pragma unroll
        for (int kk = 0; kk < 4; kk++)
#pragma unroll
          for (int t = 0; t < 2; t++) {
            const v2u v_ = trd(a_fg + e * 16384 + kk * 4096 + t * 128);
            af[4 * e + kk][2 * t] = v_.x;
            af[4 * e + kk][2 * t + 1] = v_.y;
          }
#pragma unroll
      for (int i = 0; i < 8; i++) {
        const v2u v_ = trd(a_fd + i * 32);
        df[i >> 1][2 * (i & 1)] = v_.x;
        df[i >> 1][2 * (i & 1) + 1] = v_.y;
      }
      f32x4 cn0 = Z4, cn1 = Z4, i0 = Z4, i1 = Z4, i2 = Z4;
      {
        f32x4 c0 = Z4, c1 = Z4;
#pragma unroll
        for (int kk = 0; kk < 4; kk++) { c0 = mfma32(af[kk], ones, c0); c1 = mfma32(af[4 + kk], ones, c1); }
#pragma unroll
        for (int kk = 0; kk < 4; kk++) { i0 = mfma32(af[kk], xq[kk], i0); i1 = mfma32(af[4 + kk], xq[kk], i1); i2 = mfma32(ones, xq[kk], i2); }
        if (n == 0) { lds_st<f32x4>(lb + L_CS + w * 128 + g4 * 32, c0); lds_st<f32x4>(lb + L_CS + w * 128 + g4 * 32 + 16, c1); }
        BAR_L();
#pragma unroll
        for (int i = 0; i < 4; i++) { cn0 += lds_ld<f32x4>(lb + L_CS + i * 128 + g4 * 32); cn1 += lds_ld<f32x4>(lb + L_CS + i * 128 + g4 * 32 + 16); }
        cn0 *= -1.0f / 512.0f;
        cn1 *= -1.0f / 512.0f;
      }
#pragma unroll
      for (int i = 0; i < 4; i++) PIN(df[i]);
      PIN(cn0); PIN(cn1); PIN(i0); PIN(i1); PIN(i2);
      ST(11);
      {
        NOUNROLL
        for (int spin = 0; spin < SPIN_CAP; spin++) {
          FENCE();
          hi_ = ld2<SC1>(rws, o_hi);
          if (!__any(hi_.y != tag.x)) break;
          SLEEP(DL_HR);
        }
        ST(10);
        const unsigned base = now32() - 0x100000u;
        unsigned d = hi_.x - base;
        d = umax(d, dppu<0x128>(d)); d = umax(d, dppu<0x124>(d)); d = umax(d, dppu<0x122>(d)); d = umax(d, dppu<0x121>(d));
        wait_until(base + umax(__builtin_amdgcn_readlane(d, 0), __builtin_amdgcn_readlane(d, 16)) + D_HB);
      }
      ST(12);
      f32x4 d0, d1, d2;
      {
        int spin = 0;
        NOUNROLL
        for (; spin < SPIN_CAP; spin++) {
          FENCE();
          v4u hp[8];
#pragma unroll
          for (int i = 0; i < 8; i++) hp[i] = ld4<SC1>(rws, vo, (i >> 1) * 1024 + (i & 1) * 4 * 4096);
          SCHED();
          unsigned m = 0;
#pragma unroll
          for (int i = 0; i < 8; i++) {
            d2 = mfma32(ones, hp[i], i ? d2 : i2);
            d0 = mfma32(af[i >> 1], hp[i], i ? d0 : i0);
            d1 = mfma32(af[4 + (i >> 1)], hp[i], i ? d1 : i1);
            m = umax(m, mx4(hp[i]));
            SCHED();
          }
          if (!__any(m == SENT)) break;
        SLEEP(DL_RT);
        }
        STN(22, spin);
      }
      ST(13);
#pragma unroll
      for (int i = 0; i < 4; i++) { d0[i] = sum_r48(opq(d0[i] + d2[0] * cn0[i])); d1[i] = sum_r48(opq(d1[i] + d2[0] * cn1[i])); }
      lds_st<v4u>(a_gw, v4u{sel(mq[0], asu(d0[0]), asu(d1[0])), sel(mq[0], asu(d0[1]), asu(d1[1])), sel(mq[0], asu(d0[2]), asu(d1[2])), sel(mq[0], asu(d0[3]), asu(d1[3]))});
      BAR_L();
      {
        const f32x4 g = lds_ld<f32x4>(a_gr), u = lds_ld<f32x4>(a_gr + 16);
        float md[4];
#pragma unroll
        for (int j = 0; j < 4; j++) md[j] = opq(fmx(sum_r48(g[j]), 0.f) * sum_r48(u[j]));
        const unsigned m0 = pk(md[0], md[1]), m1 = pk(md[2], md[3]);
        const v4u b0 = {m0 & mq[0], m1 & mq[0], m0 & mq[1], m1 & mq[1]}, b1 = {m0 & mq[2], m1 & mq[2], m0 & mq[3], m1 & mq[3]};
        f32x4 e0 = mfma32(df[0], b0, Z4), e1 = mfma32(df[2], b0, Z4);
        e0 = mfma32(df[1], b1, e0);
        e1 = mfma32(df[3], b1, e1);
        st4(rws, san4(asu(e0[0]), asu(e0[1]), asu(e0[2]), asu(e0[3])), o_fo);
        st4(rws, san4(asu(e1[0]), asu(e1[1]), asu(e1[2]), asu(e1[3])), o_fo, 1024);
      }
      ST(16);
    }
   }
  }
}
}

extern "C" void kernel_launch(void* const* d_in, const int* in_sizes, int n_in, void* d_out, int out_size, void* d_ws, size_t ws_size,
                              hipStream_t stream) {
  mla_reglu_decode<<<256, 512, 0, stream>>>((const u16*)d_in[0], (const u16*)d_in[1], (const u16*)d_in[2], (const u16*)d_in[3],
                                            (const u16*)d_in[4], (const u16*)d_in[5], (const u16*)d_in[7], (const u16*)d_in[8],
                                            (const u16*)d_in[9], (u16*)d_out, (unsigned char*)d_ws);
}
```
